```python
import torch

def _softmax(x, axis=-1):
    return torch.softmax(x, dim=axis)
def _gelu(x):
    return torch.nn.functional.gelu(x, approximate="tanh")
def _silu(x):
    return torch.nn.functional.silu(x)
def _sigmoid(x):
    return torch.sigmoid(x)
def _layernorm(x, axis=-1, eps=1e-5):
    m = x.mean(dim=axis, keepdim=True)
    v = x.var(dim=axis, keepdim=True, unbiased=False)
    return (x - m) / torch.sqrt(v + eps)
def _scatter_add(tgt, idx, src, axis=0):
    return tgt.clone().index_add_(axis, idx, src)
def _segment_sum(src, seg, n):
    out = torch.zeros((n,) + src.shape[1:], dtype=src.dtype)
    return out.index_add_(0, seg, src)
def _pad(x, pw):
    flat = tuple(v for pair in reversed(pw) for v in pair)
    return torch.nn.functional.pad(x, flat)

kernel_name = 'recurrent_scan__sliding_local_dilated__dense_activation_gelu__prompt'

def setup_inputs():

    _w = (lambda a: (lambda t: t.to(torch.bfloat16) if t.is_floating_point() else t)(torch.as_tensor(a)))
    def _A_setup():
        B, S, D = 1, 2048, 576
        return {
            "x": _w((lambda *s: torch.randn(*s, dtype=torch.bfloat16) * 0.05)(B, S, D)),
            "wqkv": _w((lambda *s: torch.randn(*s, dtype=torch.bfloat16) * 0.2)(D, 3 * D)),
            "wo": _w((lambda *s: torch.randn(*s, dtype=torch.bfloat16) * 0.2)(D, D)),
        }
    _A_in = _A_setup()

    def _B_setup():
        B, S, D, F = 1, 2048, 576, 4608
        return {
            "x": _w((lambda *s: torch.randn(*s, dtype=torch.bfloat16) * 0.05)(B, S, D)),
            "w1": _w((lambda *s: torch.randn(*s, dtype=torch.bfloat16) * 0.8)(D, F) * (D ** -0.5)),
            "b1": _w((lambda *s: torch.randn(*s, dtype=torch.bfloat16) * 0.8)(F) * 0.01),
            "w2": _w((lambda *s: torch.randn(*s, dtype=torch.bfloat16) * 0.1)(F, D) * (F ** -0.5)),
            "b2": _w((lambda *s: torch.randn(*s, dtype=torch.bfloat16) * 0.1)(D) * 0.01),
        }
    _B_in = _B_setup()

    return {
        "A_x": _A_in["x"],
        "A_wqkv": _A_in["wqkv"],
        "A_wo": _A_in["wo"],
        "B_x": _B_in["x"],
        "B_w1": _B_in["w1"],
        "B_b1": _B_in["b1"],
        "B_w2": _B_in["w2"],
        "B_b2": _B_in["b2"],
    }

def reference(A_x, A_wqkv, A_wo, B_x, B_w1, B_b1, B_w2, B_b2):
    def _A_ref(x, wqkv, wo):
        B, S, D = x.shape
        H = 32; Dh = D // H
        W, dil = 8, 2
        qkv = (lambda x, *d: x.permute(*d))((x @ wqkv).reshape(B, S, 3, H, Dh), 2, 0, 3, 1, 4)
        q, k, v = qkv[0], qkv[1], qkv[2]
        scale = Dh ** -0.5
        pos = torch.arange(S)
        off = torch.arange(W) * dil
        idx = pos[:, None] - off[None, :]
        mask = idx >= 0
        idx_c = torch.where(mask, idx, 0).reshape(-1)
        kg = k[:, :, idx_c, :].reshape(B, H, S, W, Dh)
        vg = v[:, :, idx_c, :].reshape(B, H, S, W, Dh)
        logits = torch.einsum("bhsd,bhswd->bhsw", q, kg) * scale
        logits = torch.where(mask[None, None, :, :], logits, -1e9)
        attn = _softmax(logits, axis=-1)
        o = torch.einsum("bhsw,bhswd->bhsd", attn, vg)
        return (lambda x, *d: x.permute(*d))(o, 0, 2, 1, 3).reshape(B, S, D) @ wo

    def _B_ref(x, w1, b1, w2, b2):
        h = torch.einsum("bsd,df->bsf", x, w1) + b1
        h = _gelu(h)
        return torch.einsum("bsf,fd->bsd", h, w2) + b2

    x = A_x
    xn = _layernorm(x, axis=-1)
    s = _A_ref(xn, A_wqkv, A_wo)
    g = _silu(_B_ref(xn, B_w1, B_b1, B_w2, B_b2))
    return x + s * g
```

```cpp
#include <hip/hip_runtime.h>
#include <hip/hip_bf16.h>

typedef __attribute__((ext_vector_type(8))) short bf16x8;
typedef __attribute__((ext_vector_type(4))) short s16x4;
typedef __attribute__((ext_vector_type(4))) float f32x4;
typedef unsigned short u16;

#define SEQ 2048
#define DM 576
#define FF 4608
#define NQKV 1728

__device__ __forceinline__ float bf2f(u16 u) { return __uint_as_float(((unsigned)u) << 16); }
__device__ __forceinline__ u16 f2bf(float f) {
  __hip_bfloat16 b = __float2bfloat16(f);
  return *reinterpret_cast<u16*>(&b);
}
__device__ __forceinline__ unsigned pack2(float a, float b) {
  return (unsigned)f2bf(a) | ((unsigned)f2bf(b) << 16);
}
__device__ __forceinline__ s16x4 tr_read(const char* p) {
  typedef __attribute__((ext_vector_type(4))) short v4i16_t;
  return __builtin_bit_cast(s16x4, __builtin_amdgcn_ds_read_tr16_b64_v4i16(
      (__attribute__((address_space(3))) v4i16_t*)p));
}
__device__ __forceinline__ bf16x8 cat8(s16x4 a, s16x4 b) {
  bf16x8 r;
  r[0] = a[0]; r[1] = a[1]; r[2] = a[2]; r[3] = a[3];
  r[4] = b[0]; r[5] = b[1]; r[6] = b[2]; r[7] = b[3];
  return r;
}

#define K1_GRP_BYTES (576 * 64 * 2)

__global__ __launch_bounds__(512, 2)
void k1_ln_gemm(const u16* __restrict__ x, const u16* __restrict__ wqkv,
                const u16* __restrict__ w1, const u16* __restrict__ b1,
                u16* __restrict__ qkv, u16* __restrict__ hbuf) {
  extern __shared__ __attribute__((aligned(16))) char smem[];
  const int tid = threadIdx.x, lane = tid & 63, wave = tid >> 6;
  const int wr = wave & 3, wc = wave >> 2;
  const int bid = blockIdx.x;
  const int rb = (bid >> 3) & 15, cs = (bid & 7) * 2 + (bid >> 7);

  const int srow = tid >> 3, scp = tid & 7;
  const int sswz = ((srow >> 1) & 1) | (((srow >> 3) & 1) << 1);
  const int sc = scp ^ (sswz << 1);

  auto stage = [&](int g, int buf) {
    const u16* base; int ld;
    if (g < 27) { base = wqkv + g * 64; ld = NQKV; } else { base = w1 + (g - 27) * 64; ld = FF; }
    const u16* p = base + (long)srow * ld + sc * 8;
    char* l = smem + buf * K1_GRP_BYTES + tid * 16;
#pragma unroll
    for (int i = 0; i < 9; ++i)
      __builtin_amdgcn_global_load_lds((const unsigned*)(p + (long)i * 64 * ld),
                                       (unsigned*)(l + i * 8192), 16, 0, 0);
  };

  stage(cs, 0);

  bf16x8 xf[2][18];
  const int fr = lane & 15, fq = lane >> 4;
#pragma unroll
  for (int rt = 0; rt < 2; ++rt) {
    const u16* xp = x + (long)(rb * 128 + wr * 32 + rt * 16 + fr) * DM + fq * 8;
#pragma unroll
    for (int s = 0; s < 18; ++s) xf[rt][s] = *reinterpret_cast<const bf16x8*>(xp + s * 32);
  }
#pragma unroll
  for (int rt = 0; rt < 2; ++rt) {
    float sum = 0.f;
#pragma unroll
    for (int s = 0; s < 18; ++s)
#pragma unroll
      for (int j = 0; j < 8; ++j) sum += bf2f((u16)xf[rt][s][j]);
    sum += __shfl_xor(sum, 16);
    sum += __shfl_xor(sum, 32);
    const float mean = sum * (1.0f / 576.0f);
    float vs = 0.f;
#pragma unroll
    for (int s = 0; s < 18; ++s)
#pragma unroll
      for (int j = 0; j < 8; ++j) { float d = bf2f((u16)xf[rt][s][j]) - mean; vs += d * d; }
    vs += __shfl_xor(vs, 16);
    vs += __shfl_xor(vs, 32);
    const float rstd = 1.0f / sqrtf(vs * (1.0f / 576.0f) + 1e-5f);
#pragma unroll
    for (int s = 0; s < 18; ++s)
#pragma unroll
      for (int j = 0; j < 8; ++j)
        xf[rt][s][j] = (short)f2bf((bf2f((u16)xf[rt][s][j]) - mean) * rstd);
    __builtin_amdgcn_sched_barrier(0);
  }

  const int q = fr >> 2, p4 = lane & 3;
  const int lswz = (q >> 1) | ((fq & 1) << 1);
  const int ng = (cs < 3) ? 7 : 6;

  for (int gi = 0; gi < ng; ++gi) {
    const int g = cs + gi * 16;
    __syncthreads();
    if (gi + 1 < ng) stage(g + 16, (gi + 1) & 1);
    const char* buf = smem + (gi & 1) * K1_GRP_BYTES;
    f32x4 acc[2][2];
#pragma unroll
    for (int t = 0; t < 2; ++t)
#pragma unroll
      for (int rt = 0; rt < 2; ++rt) acc[t][rt] = f32x4{0.f, 0.f, 0.f, 0.f};
#pragma unroll
    for (int s = 0; s < 18; ++s) {
#pragma unroll
      for (int t = 0; t < 2; ++t) {
        const int nt = wc * 2 + t;
        const char* a = buf + (32 * s + 8 * fq + q) * 128 + ((nt ^ lswz) * 32) + 8 * p4;
        bf16x8 wf = cat8(tr_read(a), tr_read(a + 512));
#pragma unroll
        for (int rt = 0; rt < 2; ++rt)
          acc[t][rt] = __builtin_amdgcn_mfma_f32_16x16x32_bf16(wf, xf[rt][s], acc[t][rt], 0, 0, 0);
      }
      if ((s % 3) == 2) __builtin_amdgcn_sched_barrier(0);
    }
#pragma unroll
    for (int t = 0; t < 2; ++t) {
      const int col = g * 64 + (wc * 2 + t) * 16 + fq * 4;
      if (g < 27) {
#pragma unroll
        for (int rt = 0; rt < 2; ++rt) {
          const int row = rb * 128 + wr * 32 + rt * 16 + fr;
          uint2 v; v.x = pack2(acc[t][rt][0], acc[t][rt][1]); v.y = pack2(acc[t][rt][2], acc[t][rt][3]);
          *reinterpret_cast<uint2*>(qkv + (long)row * NQKV + col) = v;
        }
      } else {
        const int fc = col - NQKV;
        const uint2 bb = *reinterpret_cast<const uint2*>(b1 + fc);
        float bv[4] = {bf2f((u16)(bb.x & 0xffff)), bf2f((u16)(bb.x >> 16)), bf2f((u16)(bb.y & 0xffff)), bf2f((u16)(bb.y >> 16))};
#pragma unroll
        for (int rt = 0; rt < 2; ++rt) {
          const int row = rb * 128 + wr * 32 + rt * 16 + fr;
          float r[4];
#pragma unroll
          for (int j = 0; j < 4; ++j) {
            float v = acc[t][rt][j] + bv[j];
            float u = 0.7978845608028654f * (v + 0.044715f * v * v * v);
            r[j] = v / (1.0f + __expf(-2.0f * u));
          }
          uint2 v; v.x = pack2(r[0], r[1]); v.y = pack2(r[2], r[3]);
          *reinterpret_cast<uint2*>(hbuf + (long)row * FF + fc) = v;
        }
      }
    }
  }
}

__global__ __launch_bounds__(256)
void k_attn(const u16* __restrict__ qkv, u16* __restrict__ o) {
  const int t = blockIdx.x * 256 + threadIdx.x;
  const int hd = t & 31, row = t >> 5;
  const unsigned* qp = reinterpret_cast<const unsigned*>(qkv + (long)row * NQKV + hd * 18);
  float q[18];
#pragma unroll
  for (int i = 0; i < 9; ++i) { unsigned u = qp[i]; q[2 * i] = bf2f((u16)(u & 0xffff)); q[2 * i + 1] = bf2f((u16)(u >> 16)); }
  const float scale = 0.23570226039551584f;
  float lg[8];
  float mx = -3.0e38f;
#pragma unroll
  for (int w = 0; w < 8; ++w) {
    const int idx = row - 2 * w;
    if (idx >= 0) {
      const unsigned* kp = reinterpret_cast<const unsigned*>(qkv + (long)idx * NQKV + 576 + hd * 18);
      float d = 0.f;
#pragma unroll
      for (int i = 0; i < 9; ++i) { unsigned u = kp[i]; d += q[2 * i] * bf2f((u16)(u & 0xffff)) + q[2 * i + 1] * bf2f((u16)(u >> 16)); }
      lg[w] = d * scale;
      mx = fmaxf(mx, lg[w]);
    } else lg[w] = -1e9f;
  }
  float sum = 0.f;
#pragma unroll
  for (int w = 0; w < 8; ++w) { lg[w] = (row - 2 * w >= 0) ? __expf(lg[w] - mx) : 0.f; sum += lg[w]; }
  const float inv = 1.0f / sum;
  float acc[18];
#pragma unroll
  for (int i = 0; i < 18; ++i) acc[i] = 0.f;
#pragma unroll
  for (int w = 0; w < 8; ++w) {
    const int idx = row - 2 * w;
    if (idx >= 0) {
      const unsigned* vp = reinterpret_cast<const unsigned*>(qkv + (long)idx * NQKV + 1152 + hd * 18);
      const float pw = lg[w] * inv;
#pragma unroll
      for (int i = 0; i < 9; ++i) { unsigned u = vp[i]; acc[2 * i] += pw * bf2f((u16)(u & 0xffff)); acc[2 * i + 1] += pw * bf2f((u16)(u >> 16)); }
    }
  }
  unsigned* op = reinterpret_cast<unsigned*>(o + (long)row * DM + hd * 18);
#pragma unroll
  for (int i = 0; i < 9; ++i) op[i] = pack2(acc[2 * i], acc[2 * i + 1]);
}

#define K2_BUF_BYTES (4 * 4736)
__device__ __forceinline__ int k2_rowoff(int row) { return (row >> 4) * 4736 + (row & 15) * 288 + ((row >> 3) & 1) * 128; }

__global__ __launch_bounds__(512, 2)
void k2_ffn2(const u16* __restrict__ hbuf, const u16* __restrict__ w2, float* __restrict__ part) {
  __shared__ __attribute__((aligned(16))) char smem[2 * K2_BUF_BYTES];
  const int tid = threadIdx.x, lane = tid & 63, wave = tid >> 6;
  const int wr = wave & 3, wc = wave >> 2;
  const int bid = blockIdx.x, xcd = bid & 7, idx = bid >> 3;
  const int ks = xcd & 3, rb = (xcd >> 2) * 8 + (idx & 7), cb = idx >> 3;
  const int fr = lane & 15, fq = lane >> 4, q = fr >> 2, p4 = lane & 3;

  const u16* wbase = w2 + (long)(ks * 1152) * DM + cb * 144;
  uint4 wreg0, wreg1, wreg2;
  const int c0r = tid / 18, c0c = tid % 18;
  const int c1r = (tid + 512) / 18, c1c = (tid + 512) % 18;
  const int c2r = (tid + 1024) / 18, c2c = (tid + 1024) % 18;
  const bool has2 = __builtin_amdgcn_readfirstlane(tid) < 128;
  const u16* wp0 = wbase + (long)c0r * DM + c0c * 8;
  const u16* wp1 = wbase + (long)c1r * DM + c1c * 8;
  const u16* wp2 = wbase + (long)c2r * DM + c2c * 8;
  const int so0 = k2_rowoff(c0r) + c0c * 16, so1 = k2_rowoff(c1r) + c1c * 16, so2 = k2_rowoff(c2r) + c2c * 16;
#define WLOAD(st) do { \
    wreg0 = *reinterpret_cast<const uint4*>(wp0 + (long)(st) * 64 * DM); \
    wreg1 = *reinterpret_cast<const uint4*>(wp1 + (long)(st) * 64 * DM); \
    if (has2) wreg2 = *reinterpret_cast<const uint4*>(wp2 + (long)(st) * 64 * DM); } while (0)
#define WSTORE(buf) do { \
    *reinterpret_cast<uint4*>(smem + (buf) * K2_BUF_BYTES + so0) = wreg0; \
    *reinterpret_cast<uint4*>(smem + (buf) * K2_BUF_BYTES + so1) = wreg1; \
    if (has2) *reinterpret_cast<uint4*>(smem + (buf) * K2_BUF_BYTES + so2) = wreg2; } while (0)
  const u16* hp = hbuf + (long)(rb * 128 + wr * 32 + fr) * FF + ks * 1152 + fq * 8;
  bf16x8 hf[2][2], hn[2][2];
#define HLOAD(st, dst) do { \
    _Pragma("unroll") for (int rt = 0; rt < 2; ++rt) \
      _Pragma("unroll") for (int k2 = 0; k2 < 2; ++k2) \
        dst[rt][k2] = *reinterpret_cast<const bf16x8*>(hp + (long)rt * 16 * FF + (st) * 64 + k2 * 32); } while (0)

  f32x4 acc[5][2];
#pragma unroll
  for (int t = 0; t < 5; ++t)
#pragma unroll
    for (int rt = 0; rt < 2; ++rt) acc[t][rt] = f32x4{0.f, 0.f, 0.f, 0.f};

  WLOAD(0); HLOAD(0, hf);
  WSTORE(0);
  WLOAD(1);
  __syncthreads();
  const int ntile = wc == 0 ? 5 : 4;
  const int t0 = wc * 5;
  for (int st = 0; st < 18; ++st) {
    const char* buf = smem + (st & 1) * K2_BUF_BYTES;
    if (st + 1 < 18) HLOAD(st + 1, hn);
#pragma unroll
    for (int k2 = 0; k2 < 2; ++k2) {
      const int row = k2 * 32 + 8 * fq + q;
      const char* a0 = buf + k2_rowoff(row) + 8 * p4 + t0 * 32;
      const char* a1 = buf + k2_rowoff(row + 4) + 8 * p4 + t0 * 32;
#pragma unroll
      for (int t = 0; t < 5; ++t) {
        if (t < ntile) {
          bf16x8 wf = cat8(tr_read(a0 + t * 32), tr_read(a1 + t * 32));
#pragma unroll
          for (int rt = 0; rt < 2; ++rt)
            acc[t][rt] = __builtin_amdgcn_mfma_f32_16x16x32_bf16(wf, hf[rt][k2], acc[t][rt], 0, 0, 0);
        }
      }
    }
    if (st + 1 < 18) {
      WSTORE((st + 1) & 1);
      if (st + 2 < 18) WLOAD(st + 2);
#pragma unroll
      for (int rt = 0; rt < 2; ++rt)
#pragma unroll
        for (int k2 = 0; k2 < 2; ++k2) hf[rt][k2] = hn[rt][k2];
    }
    __syncthreads();
  }
  float* pp = part + (long)ks * SEQ * DM;
#pragma unroll
  for (int t = 0; t < 5; ++t) {
    if (t < ntile) {
#pragma unroll
      for (int rt = 0; rt < 2; ++rt) {
        const int row = rb * 128 + wr * 32 + rt * 16 + fr;
        const int col = cb * 144 + (t0 + t) * 16 + fq * 4;
        *reinterpret_cast<f32x4*>(pp + (long)row * DM + col) = acc[t][rt];
      }
    }
  }
}

__global__ __launch_bounds__(512, 2)
void k3_out(const u16* __restrict__ o, const u16* __restrict__ wo, const float* __restrict__ part,
            const u16* __restrict__ b2, const u16* __restrict__ x, u16* __restrict__ out) {
  extern __shared__ __attribute__((aligned(16))) char smem[];
  const int tid = threadIdx.x, lane = tid & 63, wave = tid >> 6;
  const int bid = blockIdx.x;
  const int rb = bid / 6, cb = bid % 6;
  const int fr = lane & 15, fq = lane >> 4, q = fr >> 2, p4 = lane & 3;
  for (int ci = tid; ci < 6912; ci += 512) {
    const int row = ci / 12, cp = ci % 12;
    const int c = cp ^ (((row >> 3) & 1) << 1);
    __builtin_amdgcn_global_load_lds((const unsigned*)(wo + (long)row * DM + cb * 96 + c * 8),
                                     (unsigned*)(smem + ci * 16), 16, 0, 0);
  }
  const int rt = wave & 3, ch = wave >> 2;
  const int row = rb * 64 + rt * 16 + fr;
  bf16x8 of[18];
#pragma unroll
  for (int s = 0; s < 18; ++s) of[s] = *reinterpret_cast<const bf16x8*>(o + (long)row * DM + s * 32 + fq * 8);
  __syncthreads();
  f32x4 acc[3];
#pragma unroll
  for (int t = 0; t < 3; ++t) acc[t] = f32x4{0.f, 0.f, 0.f, 0.f};
  const int lsw = fq & 1;
#pragma unroll
  for (int s = 0; s < 18; ++s) {
#pragma unroll
    for (int t = 0; t < 3; ++t) {
      const int nt = ch * 3 + t;
      const char* a = smem + (32 * s + 8 * fq + q) * 192 + ((nt ^ lsw) * 32) + 8 * p4;
      bf16x8 wf = cat8(tr_read(a), tr_read(a + 4 * 192));
      acc[t] = __builtin_amdgcn_mfma_f32_16x16x32_bf16(wf, of[s], acc[t], 0, 0, 0);
    }
  }
#pragma unroll
  for (int t = 0; t < 3; ++t) {
    const int col = cb * 96 + (ch * 3 + t) * 16 + fq * 4;
    const long off = (long)row * DM + col;
    f32x4 y = *reinterpret_cast<const f32x4*>(part + off);
#pragma unroll
    for (int k = 1; k < 4; ++k) y += *reinterpret_cast<const f32x4*>(part + (long)k * SEQ * DM + off);
    const uint2 bb = *reinterpret_cast<const uint2*>(b2 + col);
    const uint2 xx = *reinterpret_cast<const uint2*>(x + off);
    float bv[4] = {bf2f((u16)(bb.x & 0xffff)), bf2f((u16)(bb.x >> 16)), bf2f((u16)(bb.y & 0xffff)), bf2f((u16)(bb.y >> 16))};
    float xv[4] = {bf2f((u16)(xx.x & 0xffff)), bf2f((u16)(xx.x >> 16)), bf2f((u16)(xx.y & 0xffff)), bf2f((u16)(xx.y >> 16))};
    float r[4];
#pragma unroll
    for (int j = 0; j < 4; ++j) {
      const float yy = y[j] + bv[j];
      const float g = yy / (1.0f + __expf(-yy));
      r[j] = xv[j] + acc[t][j] * g;
    }
    uint2 v; v.x = pack2(r[0], r[1]); v.y = pack2(r[2], r[3]);
    *reinterpret_cast<uint2*>(out + off) = v;
  }
}

extern "C" void kernel_launch(void* const* d_in, const int* in_sizes, int n_in,
                              void* d_out, int out_size, void* d_ws, size_t ws_size,
                              hipStream_t stream) {
  const u16* x = (const u16*)d_in[0];
  const u16* wqkv = (const u16*)d_in[1];
  const u16* wo = (const u16*)d_in[2];
  const u16* w1 = (const u16*)d_in[4];
  const u16* b1 = (const u16*)d_in[5];
  const u16* w2 = (const u16*)d_in[6];
  const u16* b2 = (const u16*)d_in[7];
  u16* out = (u16*)d_out;
  char* ws = (char*)d_ws;
  u16* qkv = (u16*)ws;
  u16* hbuf = (u16*)(ws + (size_t)SEQ * NQKV * 2);
  u16* o = (u16*)(ws + (size_t)SEQ * NQKV * 2 + (size_t)SEQ * FF * 2);
  float* part = (float*)(ws + (size_t)SEQ * NQKV * 2 + (size_t)SEQ * FF * 2 + (size_t)SEQ * DM * 2);

  hipFuncSetAttribute((const void*)k1_ln_gemm, hipFuncAttributeMaxDynamicSharedMemorySize, 2 * K1_GRP_BYTES);
  hipFuncSetAttribute((const void*)k3_out, hipFuncAttributeMaxDynamicSharedMemorySize, 576 * 192);
  k1_ln_gemm<<<256, 512, 2 * K1_GRP_BYTES, stream>>>(x, wqkv, w1, b1, qkv, hbuf);
  k_attn<<<256, 256, 0, stream>>>(qkv, o);
  k2_ffn2<<<256, 512, 0, stream>>>(hbuf, w2, part);
  k3_out<<<192, 512, 576 * 192, stream>>>(o, wo, part, b2, x, out);
}
```

```cpp
#include <hip/hip_runtime.h>
#include <hip/hip_bf16.h>

typedef __attribute__((ext_vector_type(8))) short bf16x8;
typedef __attribute__((ext_vector_type(4))) short s16x4;
typedef __attribute__((ext_vector_type(4))) float f32x4;
typedef unsigned short u16;

#define STAMP_INIT()
#define STAMP()
#define KEEP(x)
#define SEQ 2048
#define DM 576
#define FF 4608
#define NQKV 1728

__device__ __forceinline__ float bf2f(u16 u) { return __uint_as_float(((unsigned)u) << 16); }
__device__ __forceinline__ u16 f2bf(float f) {
  __hip_bfloat16 b = __float2bfloat16(f);
  return *reinterpret_cast<u16*>(&b);
}
__device__ __forceinline__ unsigned pack2(float a, float b) {
  return (unsigned)f2bf(a) | ((unsigned)f2bf(b) << 16);
}
__device__ __forceinline__ s16x4 tr_read(const char* p) {
  typedef __attribute__((ext_vector_type(4))) short v4i16_t;
  return __builtin_bit_cast(s16x4, __builtin_amdgcn_ds_read_tr16_b64_v4i16(
      (__attribute__((address_space(3))) v4i16_t*)p));
}
__device__ __forceinline__ bf16x8 cat8(s16x4 a, s16x4 b) {
  bf16x8 r;
  r[0] = a[0]; r[1] = a[1]; r[2] = a[2]; r[3] = a[3];
  r[4] = b[0]; r[5] = b[1]; r[6] = b[2]; r[7] = b[3];
  return r;
}

__device__ __forceinline__ void glds16(const void* gsrc, unsigned lds_dst) {
  unsigned keep;
  asm volatile("s_mov_b32 %0, m0\n\ts_mov_b32 m0, %2\n\ts_nop 0\n\tglobal_load_lds_dwordx4 %1, off\n\ts_mov_b32 m0, %0"
               : "=&s"(keep) : "v"(gsrc), "s"(lds_dst) : "memory");
}

__global__ __launch_bounds__(256)
void k0_ln(const u16* __restrict__ x, u16* __restrict__ xn) {
  const int lane = threadIdx.x & 63;
  const int row = blockIdx.x * 4 + (threadIdx.x >> 6);
  const u16* xp = x + (long)row * DM;
  const bool ex = lane < 8;
  bf16x8 a = *reinterpret_cast<const bf16x8*>(xp + lane * 8);
  bf16x8 b = a;
  if (ex) b = *reinterpret_cast<const bf16x8*>(xp + 512 + lane * 8);
  float fa[8], fb[8];
  float sum = 0.f;
#pragma unroll
  for (int j = 0; j < 8; ++j) { fa[j] = bf2f((u16)a[j]); fb[j] = ex ? bf2f((u16)b[j]) : 0.f; sum += fa[j] + fb[j]; }
#pragma unroll
  for (int m = 32; m >= 1; m >>= 1) sum += __shfl_xor(sum, m);
  const float mean = sum * (1.0f / 576.0f);
  float vs = 0.f;
#pragma unroll
  for (int j = 0; j < 8; ++j) { float d = fa[j] - mean; vs += d * d; if (ex) { float e = fb[j] - mean; vs += e * e; } }
#pragma unroll
  for (int m = 32; m >= 1; m >>= 1) vs += __shfl_xor(vs, m);
  const float rstd = 1.0f / sqrtf(vs * (1.0f / 576.0f) + 1e-5f);
  uint4 o;
  o.x = pack2((fa[0] - mean) * rstd, (fa[1] - mean) * rstd); o.y = pack2((fa[2] - mean) * rstd, (fa[3] - mean) * rstd);
  o.z = pack2((fa[4] - mean) * rstd, (fa[5] - mean) * rstd); o.w = pack2((fa[6] - mean) * rstd, (fa[7] - mean) * rstd);
  u16* xo = xn + (long)(row >> 5) * 36 * 512 + (row & 31) * 8;
  *reinterpret_cast<uint4*>(xo + (lane >> 1) * 512 + (lane & 1) * 256) = o;
  if (ex) {
    o.x = pack2((fb[0] - mean) * rstd, (fb[1] - mean) * rstd); o.y = pack2((fb[2] - mean) * rstd, (fb[3] - mean) * rstd);
    o.z = pack2((fb[4] - mean) * rstd, (fb[5] - mean) * rstd); o.w = pack2((fb[6] - mean) * rstd, (fb[7] - mean) * rstd);
    *reinterpret_cast<uint4*>(xo + (32 + (lane >> 1)) * 512 + (lane & 1) * 256) = o;
  }
}

typedef __attribute__((ext_vector_type(16))) float f32x16;
typedef __attribute__((ext_vector_type(2))) float f32x2;
#define K1_GRP_BYTES (576 * 32 * 2)
#define K1_LDS (3 * K1_GRP_BYTES + FF * 4)

__global__ __launch_bounds__(512, 2)
void k1_gemm(const u16* __restrict__ xn, const u16* __restrict__ wqkv,
             const u16* __restrict__ w1, const u16* __restrict__ b1,
             u16* __restrict__ qkv, u16* __restrict__ hbuf) {
  extern __shared__ __attribute__((aligned(16))) char smem[];
  STAMP_INIT();
  STAMP();
  const int tid = threadIdx.x, lane = tid & 63, wave = tid >> 6;
  const int bid = blockIdx.x;
  const int rb = (bid >> 3) & 7, cs = (bid & 7) * 4 + (bid >> 6);

  const unsigned lds0 = (unsigned)(size_t)smem;
  const int wave_u = __builtin_amdgcn_readfirstlane(wave);
  const int srow = tid >> 2, sc = tid & 3;

  auto stage = [&](int g, int slot) {
    const u16* base; int ld;
    if (g < 54) { base = wqkv + g * 32; ld = NQKV; } else { base = w1 + (g - 54) * 32; ld = FF; }
    const u16* p = base + (long)srow * ld + sc * 8;
    const unsigned l = lds0 + slot * K1_GRP_BYTES + wave_u * 1024;
#pragma unroll
    for (int i = 0; i < 4; ++i) glds16(p + (long)i * 128 * ld, l + i * 8192);
    if (wave_u < 4) glds16(p + (long)4 * 128 * ld, l + 4 * 8192);
  };
  const int ng = (cs < 6) ? 7 : 6;
  stage(cs, 0);
  stage(cs + 32, 1);

  bf16x8 xf[36];
#pragma unroll
  for (int s = 0; s < 36; ++s)
    xf[s] = *reinterpret_cast<const bf16x8*>(xn + ((long)(rb * 8 + wave) * 36 + s) * 512 + lane * 8);

  float* b1s = reinterpret_cast<float*>(smem + 3 * K1_GRP_BYTES);
  for (int i = tid; i < FF / 4; i += 512) {
    const uint2 bb = *reinterpret_cast<const uint2*>(b1 + i * 4);
    f32x4 f; f[0] = bf2f((u16)(bb.x & 0xffff)); f[1] = bf2f((u16)(bb.x >> 16)); f[2] = bf2f((u16)(bb.y & 0xffff)); f[3] = bf2f((u16)(bb.y >> 16));
    *reinterpret_cast<f32x4*>(b1s + i * 4) = f;
  }

  const int hh = lane >> 5, blk = (lane >> 4) & 1, q = (lane & 15) >> 2, p4 = lane & 3;
  const int lofs = (8 * hh + q) * 64 + blk * 32 + 8 * p4;
  const int m = lane & 31;
  const long row = rb * 256 + wave * 32 + m;
  u16* qrow = qkv + row * NQKV + 4 * hh;
  u16* hrow = hbuf + ((long)(row >> 4) * 144) * 512 + (row & 15) * 8 + 4 * hh;

  f32x16 accp;
#pragma unroll
  for (int i = 0; i < 16; ++i) accp[i] = 0.f;

  int slot = 0;
  for (int gi = 0; gi <= ng; ++gi) {
    const int g = cs + gi * 32;
    f32x16 acc;
    auto mma = [&]() {
      if (g >= 54) {
        const float* bp = b1s + (g - 54) * 32 + 4 * hh;
#pragma unroll
        for (int a = 0; a < 4; ++a) {
          const f32x4 bv = *reinterpret_cast<const f32x4*>(bp + 8 * a);
          acc[4 * a] = bv[0]; acc[4 * a + 1] = bv[1]; acc[4 * a + 2] = bv[2]; acc[4 * a + 3] = bv[3];
        }
      } else {
#pragma unroll
        for (int i = 0; i < 16; ++i) acc[i] = 0.f;
      }
      const char* buf = smem + slot * K1_GRP_BYTES + lofs;
      bf16x8 wf[8];
#pragma unroll
      for (int s = 0; s < 7; ++s) wf[s] = cat8(tr_read(buf + s * 1024), tr_read(buf + s * 1024 + 256));
#pragma unroll
      for (int s = 0; s < 36; ++s) {
        if (s + 7 < 36) wf[(s + 7) & 7] = cat8(tr_read(buf + (s + 7) * 1024), tr_read(buf + (s + 7) * 1024 + 256));
        acc = __builtin_amdgcn_mfma_f32_32x32x16_bf16(wf[s & 7], xf[s], acc, 0, 0, 0);
        __builtin_amdgcn_sched_barrier(0);
      }
    };
    auto epi = [&]() {
      const int gp = g - 32;
      if (gp < 54) {
#pragma unroll
        for (int a = 0; a < 4; ++a) {
          uint2 v; v.x = pack2(accp[4 * a], accp[4 * a + 1]); v.y = pack2(accp[4 * a + 2], accp[4 * a + 3]);
          *reinterpret_cast<uint2*>(qrow + gp * 32 + 8 * a) = v;
        }
      } else {
#pragma unroll
        for (int a = 0; a < 4; ++a) {
          unsigned w[2];
#pragma unroll
          for (int e = 0; e < 2; ++e) {
            f32x2 v = {accp[4 * a + 2 * e], accp[4 * a + 2 * e + 1]};
            f32x2 c1 = {-2.302208198f, -2.302208198f}, c2 = {-0.1029432397f, -0.1029432397f}, one = {1.0f, 1.0f};
            f32x2 u = v * (v * v * c2 + c1);
            f32x2 ex = {__builtin_amdgcn_exp2f(u[0]), __builtin_amdgcn_exp2f(u[1])};
            f32x2 d = ex + one;
            f32x2 r = {__builtin_amdgcn_rcpf(d[0]), __builtin_amdgcn_rcpf(d[1])};
            f32x2 o = v * r;
            w[e] = pack2(o[0], o[1]);
          }
          uint2 v; v.x = w[0]; v.y = w[1];
          *reinterpret_cast<uint2*>(hrow + (long)(gp - 54) * 512 + a * 128) = v;
        }
      }
    };
    if (gi < ng) {
      if (gi >= 3 && gi + 1 < ng) asm volatile("s_waitcnt vmcnt(12)" ::: "memory");
      else asm volatile("s_waitcnt vmcnt(0)" ::: "memory");
      STAMP();
      __builtin_amdgcn_s_barrier();
      STAMP();
      if (gi + 2 < ng) { int s2 = slot + 2; if (s2 >= 3) s2 -= 3; stage(g + 64, s2); }
    }
    if (wave_u < 4) {
      if (gi < ng) mma();
      KEEP(acc); STAMP();
      if (gi > 0) epi();
    } else {
      if (gi > 0) epi();
      STAMP();
      if (gi < ng) mma();
      KEEP(acc);
    }
    slot = slot + 1; if (slot >= 3) slot = 0;
    accp = acc;
  }
}

typedef __attribute__((ext_vector_type(2))) __bf16 bf2_t;
#define AT_LDS (30 * 2304)
__device__ __forceinline__ void attn_body(const u16* __restrict__ qkv, u16* __restrict__ o, int ab, char* smem) {
  const int tid = threadIdx.x;
  const int r0 = ab * 16;
  for (int ci = tid; ci < 30 * 144; ci += 512) {
    const int lr = ci / 144, c = ci % 144;
    int gr = r0 - 14 + lr; gr = gr < 0 ? 0 : gr;
    *reinterpret_cast<uint4*>(smem + lr * 2304 + c * 16) = *reinterpret_cast<const uint4*>(qkv + (long)gr * NQKV + 576 + c * 8);
  }
  const int hd = tid & 31, lrow = tid >> 5, row = r0 + lrow;
  unsigned qv[9];
  {
    const unsigned* qp = reinterpret_cast<const unsigned*>(qkv + (long)row * NQKV + hd * 18);
#pragma unroll
    for (int i = 0; i < 9; ++i) qv[i] = qp[i];
  }
  __syncthreads();
  const float scale = 0.23570226039551584f * 1.4426950408889634f;
  float lg[8];
#pragma unroll
  for (int w = 0; w < 8; ++w) {
    const unsigned* kp = reinterpret_cast<const unsigned*>(smem + (lrow + 14 - 2 * w) * 2304 + hd * 36);
    float d = 0.f;
#pragma unroll
    for (int i = 0; i < 9; ++i) d = __builtin_amdgcn_fdot2_f32_bf16(__builtin_bit_cast(bf2_t, qv[i]), __builtin_bit_cast(bf2_t, kp[i]), d, false);
    lg[w] = (row - 2 * w) < 0 ? -1e30f : d * scale;
  }
  float mx = lg[0];
#pragma unroll
  for (int w = 1; w < 8; ++w) mx = fmaxf(mx, lg[w]);
  float sum = 0.f;
#pragma unroll
  for (int w = 0; w < 8; ++w) { lg[w] = __builtin_amdgcn_exp2f(lg[w] - mx); sum += lg[w]; }
  const float inv = 1.0f / sum;
  f32x2 acc[9];
#pragma unroll
  for (int i = 0; i < 9; ++i) acc[i] = f32x2{0.f, 0.f};
#pragma unroll
  for (int w = 0; w < 8; ++w) {
    const unsigned* vp = reinterpret_cast<const unsigned*>(smem + (lrow + 14 - 2 * w) * 2304 + 1152 + hd * 36);
    const float pw = (row - 2 * w) < 0 ? 0.f : lg[w] * inv;
    const f32x2 p2 = {pw, pw};
#pragma unroll
    for (int i = 0; i < 9; ++i) {
      const unsigned u = vp[i];
      const f32x2 v2 = {__uint_as_float(u << 16), __uint_as_float(u & 0xffff0000u)};
      acc[i] += p2 * v2;
    }
  }
  u16* obase = o + (long)(row >> 4) * 18 * 512 + (row & 15) * 8;
#pragma unroll
  for (int i = 0; i < 9; ++i) {
    const int c = hd * 18 + 2 * i;
    *reinterpret_cast<unsigned*>(obase + (c >> 5) * 512 + ((c >> 3) & 3) * 128 + (c & 7)) = pack2(acc[i][0], acc[i][1]);
  }
}

#define K2_BUF_BYTES (4 * 4736)
__device__ __forceinline__ int k2_rowoff(int row) { return (row >> 4) * 4736 + (row & 15) * 288 + ((row >> 3) & 1) * 128; }

__global__ __launch_bounds__(512, 4)
void k2_ffn2(const u16* __restrict__ hbuf, const u16* __restrict__ w2, float* __restrict__ part,
             const u16* __restrict__ qkv, u16* __restrict__ o) {
  __shared__ __attribute__((aligned(16))) char smem[AT_LDS];
  if (blockIdx.x >= 256) { attn_body(qkv, o, blockIdx.x - 256, smem); return; }
  const int tid = threadIdx.x, lane = tid & 63, wave = tid >> 6;
  const int wr = wave & 3, wc = wave >> 2;
  const int bid = blockIdx.x, xcd = bid & 7, idx = bid >> 3;
  const int ks = xcd & 3, rb = (xcd >> 2) * 8 + (idx & 7), cb = idx >> 3;
  const int fr = lane & 15, fq = lane >> 4, q = fr >> 2, p4 = lane & 3;

  const u16* wbase = w2 + (long)(ks * 1152) * DM + cb * 144;
  uint4 wreg0, wreg1, wreg2;
  const int c0r = tid / 18, c0c = tid % 18;
  const int c1r = (tid + 512) / 18, c1c = (tid + 512) % 18;
  const int c2r = (tid + 1024) / 18, c2c = (tid + 1024) % 18;
  const bool has2 = __builtin_amdgcn_readfirstlane(tid) < 128;
  const u16* wp0 = wbase + (long)c0r * DM + c0c * 8;
  const u16* wp1 = wbase + (long)c1r * DM + c1c * 8;
  const u16* wp2 = wbase + (long)c2r * DM + c2c * 8;
  const int so0 = k2_rowoff(c0r) + c0c * 16, so1 = k2_rowoff(c1r) + c1c * 16, so2 = k2_rowoff(c2r) + c2c * 16;
#define WLOAD(st) do { \
    wreg0 = *reinterpret_cast<const uint4*>(wp0 + (long)(st) * 64 * DM); \
    wreg1 = *reinterpret_cast<const uint4*>(wp1 + (long)(st) * 64 * DM); \
    if (has2) wreg2 = *reinterpret_cast<const uint4*>(wp2 + (long)(st) * 64 * DM); } while (0)
#define WSTORE(buf) do { \
    *reinterpret_cast<uint4*>(smem + (buf) * K2_BUF_BYTES + so0) = wreg0; \
    *reinterpret_cast<uint4*>(smem + (buf) * K2_BUF_BYTES + so1) = wreg1; \
    if (has2) *reinterpret_cast<uint4*>(smem + (buf) * K2_BUF_BYTES + so2) = wreg2; } while (0)
  const u16* hp = hbuf + ((long)(rb * 8 + wr * 2) * 144 + ks * 36) * 512 + lane * 8;
  bf16x8 hf[2][2], hn[2][2];
#define HLOAD(st, dst) do { \
    _Pragma("unroll") for (int rt = 0; rt < 2; ++rt) \
      _Pragma("unroll") for (int k2 = 0; k2 < 2; ++k2) \
        dst[rt][k2] = *reinterpret_cast<const bf16x8*>(hp + ((long)rt * 144 + (st) * 2 + k2) * 512); } while (0)

  f32x4 acc[5][2];
#pragma unroll
  for (int t = 0; t < 5; ++t)
#pragma unroll
    for (int rt = 0; rt < 2; ++rt) acc[t][rt] = f32x4{0.f, 0.f, 0.f, 0.f};

  WLOAD(0); HLOAD(0, hf);
  WSTORE(0);
  WLOAD(1);
  __syncthreads();
  const int ntile = wc == 0 ? 5 : 4;
  const int t0 = wc * 5;
  for (int st = 0; st < 18; ++st) {
    const char* buf = smem + (st & 1) * K2_BUF_BYTES;
    if (st + 1 < 18) HLOAD(st + 1, hn);
#pragma unroll
    for (int k2 = 0; k2 < 2; ++k2) {
      const int row = k2 * 32 + 8 * fq + q;
      const char* a0 = buf + k2_rowoff(row) + 8 * p4 + t0 * 32;
      const char* a1 = buf + k2_rowoff(row + 4) + 8 * p4 + t0 * 32;
#pragma unroll
      for (int t = 0; t < 5; ++t) {
        if (t < ntile) {
          bf16x8 wf = cat8(tr_read(a0 + t * 32), tr_read(a1 + t * 32));
#pragma unroll
          for (int rt = 0; rt < 2; ++rt)
            acc[t][rt] = __builtin_amdgcn_mfma_f32_16x16x32_bf16(wf, hf[rt][k2], acc[t][rt], 0, 0, 0);
        }
      }
    }
    if (st + 1 < 18) {
      WSTORE((st + 1) & 1);
      if (st + 2 < 18) WLOAD(st + 2);
#pragma unroll
      for (int rt = 0; rt < 2; ++rt)
#pragma unroll
        for (int k2 = 0; k2 < 2; ++k2) hf[rt][k2] = hn[rt][k2];
    }
    __syncthreads();
  }
  float* pp = part + (long)ks * SEQ * DM;
#pragma unroll
  for (int t = 0; t < 5; ++t) {
    if (t < ntile) {
#pragma unroll
      for (int rt = 0; rt < 2; ++rt) {
        const int R = rb * 8 + wr * 2 + rt, T = cb * 9 + t0 + t;
        *reinterpret_cast<f32x4*>(pp + ((long)(R * 36 + T) * 64 + lane) * 4) = acc[t][rt];
      }
    }
  }
}

__global__ __launch_bounds__(512, 2)
void k3_out(const u16* __restrict__ o, const u16* __restrict__ wo, const float* __restrict__ part,
            const u16* __restrict__ b2, const u16* __restrict__ x, u16* __restrict__ out) {
  extern __shared__ __attribute__((aligned(16))) char smem[];
  const int tid = threadIdx.x, lane = tid & 63, wave = tid >> 6;
  const int bid = blockIdx.x;
  const int rb = bid / 6, cb = bid % 6;
  const int fr = lane & 15, fq = lane >> 4, q = fr >> 2, p4 = lane & 3;
  const unsigned lds0 = (unsigned)(size_t)smem;
  const int wave_u = __builtin_amdgcn_readfirstlane(wave);
#pragma unroll
  for (int i = 0; i < 14; ++i) {
    const int ci = tid + i * 512;
    if (i < 13 || wave_u < 4) {
      const int row = ci / 12, cp = ci % 12;
      const int c = cp ^ (((row >> 3) & 1) << 1);
      glds16(wo + (long)row * DM + cb * 96 + c * 8, lds0 + (wave_u * 64 + i * 512) * 16);
    }
  }
  const int rt = wave & 3, ch = wave >> 2;
  const int row = rb * 64 + rt * 16 + fr;
  bf16x8 of[18];
#pragma unroll
  for (int s = 0; s < 18; ++s) of[s] = *reinterpret_cast<const bf16x8*>(o + ((long)(rb * 4 + rt) * 18 + s) * 512 + lane * 8);
  f32x4 y[3]; uint2 bb[3], xx[3];
#pragma unroll
  for (int t = 0; t < 3; ++t) {
    const int col = cb * 96 + (ch * 3 + t) * 16 + fq * 4;
    const long off = (long)row * DM + col;
    const long po = ((long)((rb * 4 + rt) * 36 + cb * 6 + ch * 3 + t) * 64 + lane) * 4;
    y[t] = *reinterpret_cast<const f32x4*>(part + po);
#pragma unroll
    for (int k = 1; k < 4; ++k) y[t] += *reinterpret_cast<const f32x4*>(part + (long)k * SEQ * DM + po);
    bb[t] = *reinterpret_cast<const uint2*>(b2 + col);
    xx[t] = *reinterpret_cast<const uint2*>(x + off);
  }
  asm volatile("s_waitcnt vmcnt(0)" ::: "memory");
  __builtin_amdgcn_s_barrier();
  f32x4 acc[3];
#pragma unroll
  for (int t = 0; t < 3; ++t) acc[t] = f32x4{0.f, 0.f, 0.f, 0.f};
  const int lsw = fq & 1;
#pragma unroll
  for (int s = 0; s < 18; ++s) {
#pragma unroll
    for (int t = 0; t < 3; ++t) {
      const int nt = ch * 3 + t;
      const char* a = smem + (32 * s + 8 * fq + q) * 192 + ((nt ^ lsw) * 32) + 8 * p4;
      bf16x8 wf = cat8(tr_read(a), tr_read(a + 4 * 192));
      acc[t] = __builtin_amdgcn_mfma_f32_16x16x32_bf16(wf, of[s], acc[t], 0, 0, 0);
    }
  }
#pragma unroll
  for (int t = 0; t < 3; ++t) {
    const int col = cb * 96 + (ch * 3 + t) * 16 + fq * 4;
    const long off = (long)row * DM + col;
    float bv[4] = {bf2f((u16)(bb[t].x & 0xffff)), bf2f((u16)(bb[t].x >> 16)), bf2f((u16)(bb[t].y & 0xffff)), bf2f((u16)(bb[t].y >> 16))};
    float xv[4] = {bf2f((u16)(xx[t].x & 0xffff)), bf2f((u16)(xx[t].x >> 16)), bf2f((u16)(xx[t].y & 0xffff)), bf2f((u16)(xx[t].y >> 16))};
    float r[4];
#pragma unroll
    for (int j = 0; j < 4; ++j) {
      const float yy = y[t][j] + bv[j];
      const float g = yy * __builtin_amdgcn_rcpf(1.0f + __expf(-yy));
      r[j] = xv[j] + acc[t][j] * g;
    }
    uint2 v; v.x = pack2(r[0], r[1]); v.y = pack2(r[2], r[3]);
    *reinterpret_cast<uint2*>(out + off) = v;
  }
}

extern "C" void kernel_launch(void* const* d_in, const int* in_sizes, int n_in,
                              void* d_out, int out_size, void* d_ws, size_t ws_size,
                              hipStream_t stream) {
  const u16* x = (const u16*)d_in[0];
  const u16* wqkv = (const u16*)d_in[1];
  const u16* wo = (const u16*)d_in[2];
  const u16* w1 = (const u16*)d_in[4];
  const u16* b1 = (const u16*)d_in[5];
  const u16* w2 = (const u16*)d_in[6];
  const u16* b2 = (const u16*)d_in[7];
  u16* out = (u16*)d_out;
  char* ws = (char*)d_ws;
  u16* qkv = (u16*)ws;
  u16* hbuf = (u16*)(ws + (size_t)SEQ * NQKV * 2);
  u16* o = (u16*)(ws + (size_t)SEQ * NQKV * 2 + (size_t)SEQ * FF * 2);
  float* part = (float*)(ws + (size_t)SEQ * NQKV * 2 + (size_t)SEQ * FF * 2 + (size_t)SEQ * DM * 2);
  u16* xn = (u16*)(ws + (size_t)SEQ * NQKV * 2 + (size_t)SEQ * FF * 2 + (size_t)SEQ * DM * 2 + (size_t)4 * SEQ * DM * 4);

  hipFuncSetAttribute((const void*)k1_gemm, hipFuncAttributeMaxDynamicSharedMemorySize, K1_LDS);
  hipFuncSetAttribute((const void*)k3_out, hipFuncAttributeMaxDynamicSharedMemorySize, 576 * 192);
  k0_ln<<<512, 256, 0, stream>>>(x, xn);
  k1_gemm<<<256, 512, K1_LDS, stream>>>(xn, wqkv, w1, b1, qkv, hbuf);
  k2_ffn2<<<384, 512, 0, stream>>>(hbuf, w2, part, qkv, o);
  k3_out<<<192, 512, 576 * 192, stream>>>(o, wo, part, b2, x, out);
}
```
